```python
import torch

def _softmax(x, axis=-1):
    return torch.softmax(x, dim=axis)
def _gelu(x):
    return torch.nn.functional.gelu(x, approximate="tanh")
def _silu(x):
    return torch.nn.functional.silu(x)
def _sigmoid(x):
    return torch.sigmoid(x)
def _layernorm(x, axis=-1, eps=1e-5):
    m = x.mean(dim=axis, keepdim=True)
    v = x.var(dim=axis, keepdim=True, unbiased=False)
    return (x - m) / torch.sqrt(v + eps)
def _scatter_add(tgt, idx, src, axis=0):
    return tgt.clone().index_add_(axis, idx, src)
def _segment_sum(src, seg, n):
    out = torch.zeros((n,) + src.shape[1:], dtype=src.dtype)
    return out.index_add_(0, seg, src)
def _pad(x, pw):
    flat = tuple(v for pair in reversed(pw) for v in pair)
    return torch.nn.functional.pad(x, flat)

kernel_name = 'recurrent_scan__sliding_block_cached__mixer_token_mlp__decode'

def setup_inputs():

    _w = (lambda a: (lambda t: t.to(torch.bfloat16) if t.is_floating_point() else t)(torch.as_tensor(a)))
    def _A_setup():
        B, Sq, D = 4, 1, 576
        Hq, Hkv, Sk, Dh = 32, 16, 8192, 128
        assert Hq % Hkv == 0, (Hq, Hkv)
        return {
            "x": _w((lambda *s: torch.randn(*s, dtype=torch.bfloat16) * 0.05)(B, Sq, D)),
            "k_cache": _w((lambda *s: torch.randn(*s, dtype=torch.bfloat16) * 1.6)(B, Hkv, Sk, Dh)),
            "v_cache": _w((lambda *s: torch.randn(*s, dtype=torch.bfloat16) * 1.6)(B, Hkv, Sk, Dh)),
            "wq": _w((lambda *s: torch.randn(*s, dtype=torch.bfloat16) * 1.6)(D, Hq * Dh) * (D ** -0.5)),
            "wo": _w((lambda *s: torch.randn(*s, dtype=torch.bfloat16) * 1.6)(Hq * Dh, D) * ((Hq * Dh) ** -0.5)),
        }
    _A_in = _A_setup()

    def _B_setup():
        B, S, D, F = 4, 1, 576, 4608
        return {
            "x": _w((lambda *s: torch.randn(*s, dtype=torch.bfloat16) * 0.05)(B, S, D)),
            "w1": _w((lambda *s: torch.randn(*s, dtype=torch.bfloat16) * 0.2)(S, F) * (S ** -0.5)),
            "b1": _w((lambda *s: torch.randn(*s, dtype=torch.bfloat16) * 0.2)(F) * 0.02),
            "w2": _w((lambda *s: torch.randn(*s, dtype=torch.bfloat16) * 0.2)(F, S) * (F ** -0.5)),
            "b2": _w((lambda *s: torch.randn(*s, dtype=torch.bfloat16) * 0.2)(S) * 0.02),
        }
    _B_in = _B_setup()

    return {
        "A_x": _A_in["x"],
        "A_k_cache": _A_in["k_cache"],
        "A_v_cache": _A_in["v_cache"],
        "A_wq": _A_in["wq"],
        "A_wo": _A_in["wo"],
        "B_x": _B_in["x"],
        "B_w1": _B_in["w1"],
        "B_b1": _B_in["b1"],
        "B_w2": _B_in["w2"],
        "B_b2": _B_in["b2"],
    }

def reference(A_x, A_k_cache, A_v_cache, A_wq, A_wo, B_x, B_w1, B_b1, B_w2, B_b2):
    def _A_ref(x, k_cache, v_cache, wq, wo):
        B, Sq, D = x.shape
        Hq, Dh = 32, 128
        Hkv, Sk = k_cache.shape[1], k_cache.shape[2]
        W = min(4096, max(256, Sk // 4))
        rep = Hq // Hkv
        q = (lambda x, *d: x.permute(*d))((x @ wq).reshape(B, Sq, Hq, Dh), 0, 2, 1, 3)
        k = (lambda x, n, axis: torch.repeat_interleave(x, n, dim=axis))(k_cache[:, :, Sk - W:, :], rep, axis=1)
        v = (lambda x, n, axis: torch.repeat_interleave(x, n, dim=axis))(v_cache[:, :, Sk - W:, :], rep, axis=1)
        scores = torch.einsum("bhqd,bhkd->bhqk", q, k) * (Dh ** -0.5)
        p = _softmax(scores, axis=-1)
        o = torch.einsum("bhqk,bhkd->bhqd", p, v)
        return (lambda x, *d: x.permute(*d))(o, 0, 2, 1, 3).reshape(B, Sq, Hq * Dh) @ wo

    def _B_ref(x, w1, b1, w2, b2):
        B, S, D = x.shape
        h = _layernorm(x, axis=-1)
        u = torch.einsum("bsd,sf->bdf", h, w1) + b1
        u = _gelu(u)
        y = torch.einsum("bdf,fs->bds", u, w2) + b2
        return (lambda x, *d: x.permute(*d))(y, 0, 2, 1)

    x = A_x
    xn = _layernorm(x, axis=-1)
    s = _A_ref(xn, A_k_cache, A_v_cache, A_wq, A_wo)
    g = _silu(_B_ref(xn, B_w1, B_b1, B_w2, B_b2))
    return x + s * g
```

```cpp
#include <hip/hip_runtime.h>
#include <hip/hip_bf16.h>
#include <stdint.h>

typedef unsigned short u16;
typedef unsigned u32x4 __attribute__((ext_vector_type(4)));
constexpr int NB = 4, D = 576, HQ = 32, HKV = 16, SK = 8192, DH = 128, W = 2048, F = 4608;

__device__ __forceinline__ float bf2f(u16 v) { return __uint_as_float((unsigned)v << 16); }
__device__ __forceinline__ float rbf(float f) { return __bfloat162float(__float2bfloat16(f)); }

__device__ float block_sum(float v, float* red) {
    for (int o = 32; o >= 1; o >>= 1) v += __shfl_xor(v, o);
    __syncthreads();
    if ((threadIdx.x & 63) == 0) red[threadIdx.x >> 6] = v;
    __syncthreads();
    float s = 0;
    for (int i = 0; i < (int)(blockDim.x >> 6); ++i) s += red[i];
    return s;
}

__device__ void ln_rows(const float* in, float* out, float* red) {
    for (int b = 0; b < NB; ++b) {
        float s = 0;
        for (int i = threadIdx.x; i < D; i += blockDim.x) s += in[b * D + i];
        float m = block_sum(s, red) / D;
        float v = 0;
        for (int i = threadIdx.x; i < D; i += blockDim.x) { float d = in[b * D + i] - m; v += d * d; }
        v = block_sum(v, red) / D;
        float r = 1.0f / sqrtf(v + 1e-5f);
        for (int i = threadIdx.x; i < D; i += blockDim.x) out[b * D + i] = rbf((in[b * D + i] - m) * r);
    }
    __syncthreads();
}

__global__ void __launch_bounds__(256) k1(const u16* x, const u16* wq, const u16* w1, const u16* b1, const u16* w2, const u16* b2,
                                          float* q, float* g) {
    __shared__ float xs[NB * D], xn[NB * D], hh[NB * D], red[8], part[16][NB][16];
    for (int i = threadIdx.x; i < NB * D; i += 256) xs[i] = bf2f(x[i]);
    __syncthreads();
    ln_rows(xs, xn, red);
    ln_rows(xn, hh, red);
    int c = threadIdx.x & 15, r0 = threadIdx.x >> 4;
    int col = blockIdx.x * 16 + c;
    float acc[NB] = {0, 0, 0, 0};
    for (int r = r0; r < D; r += 16) {
        float w = bf2f(wq[(size_t)r * (HQ * DH) + col]);
        for (int b = 0; b < NB; ++b) acc[b] += xn[b * D + r] * w;
    }
    for (int b = 0; b < NB; ++b) part[r0][b][c] = acc[b];
    __syncthreads();
    if (threadIdx.x < 64) {
        int b = threadIdx.x >> 4, cc = threadIdx.x & 15;
        float s = 0;
        for (int i = 0; i < 16; ++i) s += part[i][b][cc];
        q[b * (HQ * DH) + blockIdx.x * 16 + cc] = rbf(s);
    }
    for (int j = 0; j < 9; ++j) {
        int idx = blockIdx.x * 9 + j;
        float h = hh[idx];
        float a = 0;
        for (int f = threadIdx.x; f < F; f += 256) {
            float u = h * bf2f(w1[f]) + bf2f(b1[f]);
            float z = 0.7978845608028654f * (u + 0.044715f * u * u * u);
            float ge = u / (1.0f + __expf(-2.0f * z));
            a += ge * bf2f(w2[f]);
        }
        a = block_sum(a, red) + bf2f(b2[0]);
        if (threadIdx.x == 0) g[idx] = a / (1.0f + __expf(-a));
    }
}

__global__ void __launch_bounds__(256) k2(const u16* kc, const u16* vc, const float* q, float* po, float* pml) {
    __shared__ float qs[2][DH], sc[2][512], red[8], ored[4][2][DH];
    int s = blockIdx.x & 3, kvh = (blockIdx.x >> 2) & 15, b = blockIdx.x >> 6;
    qs[threadIdx.x >> 7][threadIdx.x & 127] = q[b * (HQ * DH) + kvh * 256 + threadIdx.x];
    __syncthreads();
    size_t base = ((size_t)(b * HKV + kvh) * SK + (SK - W) + s * 512) * DH;
    int lane = threadIdx.x & 63, wv = threadIdx.x >> 6, g4 = lane >> 4, c = lane & 15;
    for (int i = 0; i < 32; ++i) {
        int key = wv * 128 + i * 4 + g4;
        u32x4 kk = *(const u32x4*)(kc + base + (size_t)key * DH + c * 8);
        float s0 = 0, s1 = 0;
        for (int j = 0; j < 4; ++j) {
            float lo = __uint_as_float(kk[j] << 16), hi = __uint_as_float(kk[j] & 0xffff0000u);
            s0 += lo * qs[0][c * 8 + 2 * j] + hi * qs[0][c * 8 + 2 * j + 1];
            s1 += lo * qs[1][c * 8 + 2 * j] + hi * qs[1][c * 8 + 2 * j + 1];
        }
        for (int o = 8; o >= 1; o >>= 1) { s0 += __shfl_xor(s0, o); s1 += __shfl_xor(s1, o); }
        if (c == 0) { sc[0][key] = s0 * 0.08838834764831845f; sc[1][key] = s1 * 0.08838834764831845f; }
    }
    __syncthreads();
    float m[2], l[2];
    for (int h = 0; h < 2; ++h) {
        float mx = -INFINITY;
        for (int i = 0; i < 512; ++i) mx = fmaxf(mx, sc[h][i]);
        m[h] = mx;
    }
    __syncthreads();
    for (int h = 0; h < 2; ++h) {
        float a = 0;
        for (int i = threadIdx.x; i < 512; i += 256) { float p = __expf(sc[h][i] - m[h]); sc[h][i] = p; a += p; }
        l[h] = block_sum(a, red);
    }
    __syncthreads();
    float o0[8] = {0}, o1[8] = {0};
    for (int i = 0; i < 32; ++i) {
        int key = wv * 128 + i * 4 + g4;
        u32x4 vv = *(const u32x4*)(vc + base + (size_t)key * DH + c * 8);
        float p0 = sc[0][key], p1 = sc[1][key];
        for (int j = 0; j < 4; ++j) {
            float lo = __uint_as_float(vv[j] << 16), hi = __uint_as_float(vv[j] & 0xffff0000u);
            o0[2 * j] += p0 * lo; o0[2 * j + 1] += p0 * hi; o1[2 * j] += p1 * lo; o1[2 * j + 1] += p1 * hi;
        }
    }
    for (int j = 0; j < 8; ++j) {
        o0[j] += __shfl_xor(o0[j], 16); o0[j] += __shfl_xor(o0[j], 32);
        o1[j] += __shfl_xor(o1[j], 16); o1[j] += __shfl_xor(o1[j], 32);
        if (g4 == 0) { ored[wv][0][c * 8 + j] = o0[j]; ored[wv][1][c * 8 + j] = o1[j]; }
    }
    __syncthreads();
    {
        int h = threadIdx.x >> 7, d = threadIdx.x & 127;
        float v = ored[0][h][d] + ored[1][h][d] + ored[2][h][d] + ored[3][h][d];
        po[(size_t)blockIdx.x * 256 + threadIdx.x] = v;
        if (d == 0) { pml[blockIdx.x * 4 + h * 2] = m[h]; pml[blockIdx.x * 4 + h * 2 + 1] = l[h]; }
    }
}

__global__ void __launch_bounds__(256) k3(const u16* x, const u16* wo, const float* po, const float* pml, const float* g, __hip_bfloat16* out) {
    __shared__ float os[NB][HQ * DH];
    __shared__ float part[16][NB][16];
    for (int i = threadIdx.x; i < NB * HQ * DH; i += 256) {
        int b = i >> 12, hd = i & 4095, kvh = hd >> 8, h = (hd >> 7) & 1;
        int blk = (b * 16 + kvh) * 4;
        float M = -INFINITY;
        for (int s = 0; s < 4; ++s) M = fmaxf(M, pml[(blk + s) * 4 + h * 2]);
        float L = 0, o = 0;
        for (int s = 0; s < 4; ++s) {
            float w = __expf(pml[(blk + s) * 4 + h * 2] - M);
            L += w * pml[(blk + s) * 4 + h * 2 + 1];
            o += w * po[(size_t)(blk + s) * 256 + (hd & 255)];
        }
        os[b][hd] = rbf(o / L);
    }
    __syncthreads();
    int c = threadIdx.x & 15, r0 = threadIdx.x >> 4;
    int col = blockIdx.x * 16 + c;
    float acc[NB] = {0, 0, 0, 0};
    for (int r = r0; r < HQ * DH; r += 16) {
        float w = bf2f(wo[(size_t)r * D + col]);
        for (int b = 0; b < NB; ++b) acc[b] += os[b][r] * w;
    }
    for (int b = 0; b < NB; ++b) part[r0][b][c] = acc[b];
    __syncthreads();
    if (threadIdx.x < 64) {
        int b = threadIdx.x >> 4, cc = threadIdx.x & 15;
        float s = 0;
        for (int i = 0; i < 16; ++i) s += part[i][b][cc];
        int idx = b * D + blockIdx.x * 16 + cc;
        out[idx] = __float2bfloat16(bf2f(x[idx]) + s * g[idx]);
    }
}

extern "C" void kernel_launch(void* const* d_in, const int* in_sizes, int n_in, void* d_out, int out_size, void* d_ws, size_t ws_size,
                              hipStream_t stream) {
    const u16* x = (const u16*)d_in[0];
    const u16* kc = (const u16*)d_in[1];
    const u16* vc = (const u16*)d_in[2];
    const u16* wq = (const u16*)d_in[3];
    const u16* wo = (const u16*)d_in[4];
    const u16* w1 = (const u16*)d_in[6];
    const u16* b1 = (const u16*)d_in[7];
    const u16* w2 = (const u16*)d_in[8];
    const u16* b2 = (const u16*)d_in[9];
    float* ws = (float*)d_ws;
    float* q = ws;
    float* g = ws + 16384;
    float* po = ws + 32768;
    float* pml = ws + 32768 + 65536;
    k1<<<256, 256, 0, stream>>>(x, wq, w1, b1, w2, b2, q, g);
    k2<<<256, 256, 0, stream>>>(kc, vc, q, po, pml);
    k3<<<36, 256, 0, stream>>>(x, wo, po, pml, g, (__hip_bfloat16*)d_out);
}
```

```cpp
#include <hip/hip_runtime.h>
#include <hip/hip_bf16.h>
#include <stdint.h>

typedef unsigned long long u64;
typedef unsigned u32x4 __attribute__((ext_vector_type(4)));
typedef unsigned u32x2 __attribute__((ext_vector_type(2)));
typedef float f32x4 __attribute__((ext_vector_type(4)));
typedef float f32x2 __attribute__((ext_vector_type(2)));
typedef short s16x4 __attribute__((ext_vector_type(4)));
typedef __bf16 bf16x2 __attribute__((ext_vector_type(2)));
typedef __bf16 bf16x8 __attribute__((ext_vector_type(8)));
#define LAS __attribute__((address_space(3)))
#define GAS __attribute__((address_space(1)))
typedef __attribute__((address_space(4))) const u64* kargp;
#define SB() __builtin_amdgcn_sched_barrier(0)

constexpr int DM = 576, SK = 8192, WIN = 2048, FF = 4608;
constexpr int AUX_NT = 2, AUX_SC1 = 16;
constexpr unsigned SENT = 0xFFFFFFFFu;
constexpr int OOB = 0x7fffff00;
constexpr unsigned SPIN_CAP = 1u << 20;
constexpr int CODE_BYTES = 16384;
constexpr int DLY_PARAM = 4, DLY_Q = 50, DLY_O = 20, DLY_ML = 8, DLY_S = 24;
constexpr int KHEAD = 3;
constexpr int WS_EPOCH = 0;
constexpr int WS_ML = 1024;
constexpr int WS_Q = 8192;
constexpr int WS_O = WS_Q + 16 * 4 * 4 * 1024;
constexpr int WS_S = WS_O + 16 * 2 * 16 * 256;
constexpr int WS_END = WS_S + 1024 * 320;
constexpr int L_FLAG = 0, L_Q = 256, L_P = L_Q + 512, L_ML = L_P + 2048, L_Y = L_ML + 64, L_WT = L_Y + 192, L_O = L_WT + 64, L_XN = L_O + 4352,
              L_WQ = L_XN + 4624, L_WO = L_WQ + 18432, L_H = L_WO + 20480, L_PR = L_H + 4096, L_END = L_PR + 3 * 9216;
constexpr int F_Q = 0, F_WQ = 4, F_TAG = 8, F_G0 = 16, F_P = 20, F_WO = 24, F_VI = 28, F_EP = 32;

__device__ __forceinline__ __amdgpu_buffer_rsrc_t mkrs(const void* p, int bytes) { return __builtin_amdgcn_make_buffer_rsrc((void*)p, 0, bytes, 0x00020000); }
__device__ __forceinline__ float lo_f(unsigned v) { return __builtin_bit_cast(float, v << 16); }
__device__ __forceinline__ float hi_f(unsigned v) { return __builtin_bit_cast(float, v & 0xffff0000u); }
__device__ __forceinline__ unsigned pk_bf16(float lo, float hi) {
    f32x2 t = {lo, hi};
    return __builtin_bit_cast(unsigned, __builtin_convertvector(t, bf16x2));
}
__device__ __forceinline__ unsigned pk20(float v, unsigned t) { return ((__float_as_uint(v) + 0x800u) & 0xfffff000u) | t; }
__device__ __forceinline__ float un20(unsigned w) { return __uint_as_float(w & 0xfffff000u); }
__device__ __forceinline__ unsigned bad20(u32x4 w, u32x2 t) { return (w.x ^ t.x) | (w.y ^ t.y) | (w.z ^ t.x) | (w.w ^ t.y); }
__device__ __forceinline__ float dot2(unsigned a, unsigned b, float c) {
    return __builtin_amdgcn_fdot2_f32_bf16(__builtin_bit_cast(bf16x2, a), __builtin_bit_cast(bf16x2, b), c, false);
}
template <int CTRL> __device__ __forceinline__ float dpp(float x) {
    return __builtin_bit_cast(float, __builtin_amdgcn_mov_dpp(__builtin_bit_cast(int, x), CTRL, 0xf, 0xf, true));
}
constexpr int XOR1 = 0xB1, XOR2 = 0x4E, MIR8 = 0x141, ROR8 = 0x128, ROR4 = 0x124;
__device__ __forceinline__ float row_sum(float x) { x += dpp<XOR1>(x); x += dpp<XOR2>(x); x += dpp<MIR8>(x); x += dpp<ROR8>(x); return x; }
__device__ __forceinline__ float swap32_sum(float a, float b) {
    auto s = __builtin_amdgcn_permlane32_swap(__float_as_uint(a), __float_as_uint(b), false, false);
    return __uint_as_float(s[0]) + __uint_as_float(s[1]);
}
__device__ __forceinline__ float swap16_sum(float a, float b) {
    auto s = __builtin_amdgcn_permlane16_swap(__float_as_uint(a), __float_as_uint(b), false, false);
    return __uint_as_float(s[0]) + __uint_as_float(s[1]);
}
__device__ __forceinline__ float xrow_sum(float x) { x = swap16_sum(x, x); return swap32_sum(x, x); }
__device__ __forceinline__ float xrow_max(float x) {
    auto s = __builtin_amdgcn_permlane16_swap(__float_as_uint(x), __float_as_uint(x), false, false);
    x = fmaxf(__uint_as_float(s[0]), __uint_as_float(s[1]));
    auto t = __builtin_amdgcn_permlane32_swap(__float_as_uint(x), __float_as_uint(x), false, false);
    return fmaxf(__uint_as_float(t[0]), __uint_as_float(t[1]));
}
__device__ __forceinline__ f32x4 mfma32(u32x4 a, u32x4 b, f32x4 c) {
    return __builtin_amdgcn_mfma_f32_16x16x32_bf16(__builtin_bit_cast(bf16x8, a), __builtin_bit_cast(bf16x8, b), c, 0, 0, 0);
}
__device__ __forceinline__ f32x4 mfma16(u32x2 a, u32x2 b, f32x4 c) {
    return __builtin_amdgcn_mfma_f32_16x16x16bf16_1k(__builtin_bit_cast(s16x4, a), __builtin_bit_cast(s16x4, b), c, 0, 0, 0);
}
__device__ __forceinline__ u32x2 tr_read(LAS unsigned char* p) {
    return __builtin_bit_cast(u32x2, __builtin_amdgcn_ds_read_tr16_b64_v4i16((LAS s16x4*)p));
}
template <int SLP> __device__ __forceinline__ void lds_wait(LAS unsigned char* p, unsigned want) {
#pragma unroll 1
    for (unsigned spins = 0; spins < SPIN_CAP * 64; ++spins) {
        if ((unsigned)__builtin_amdgcn_readfirstlane(*(volatile LAS unsigned*)p) >= want) break;
        if (SLP) __builtin_amdgcn_s_sleep(SLP);
    }
    asm volatile("" ::: "memory");
}
__device__ __forceinline__ void lds_signal(LAS unsigned char* p, int lane) {
    asm volatile("s_waitcnt lgkmcnt(0)" ::: "memory");
    if (lane == 0) asm volatile("ds_add_u32 %0, %1" :: "v"((unsigned)(size_t)p), "v"(1u) : "memory");
}
#define MEMBAR() asm volatile("" ::: "memory")
#define WG_BARRIER() asm volatile("s_waitcnt lgkmcnt(0)\n\ts_barrier" ::: "memory")
#define DRAIN() asm volatile("s_waitcnt vmcnt(0)" ::: "memory")

__device__ __forceinline__ void out_tile(LAS unsigned char* L, __amdgpu_buffer_rsrc_t rw, int t5, int g, int c, int so, u32x2 tag) {
    LAS unsigned char* wb = L + L_WO + (g * 8 + (c >> 2)) * 144 + (c & 3) * 8 + t5 * 32;
    unsigned ob = L_O + c * 272 + g * 16, wa = L_WT + g * 16;
    lds_wait<0>(L + L_FLAG + F_WO, 4u);
    u32x4 bf[4], af[4];
#pragma unroll
    for (int s = 0; s < 4; ++s) {
        const u32x2 b0 = tr_read(wb + s * 32 * 144), b1 = tr_read(wb + (s * 32 + 4) * 144);
        bf[s] = u32x4{b0.x, b0.y, b1.x, b1.y};
    }
    asm volatile("" : "+v"(bf[0]), "+v"(bf[1]), "+v"(bf[2]), "+v"(bf[3]), "+v"(ob), "+v"(wa));
    WG_BARRIER();
#pragma unroll
    for (int s = 0; s < 4; ++s) af[s] = *(LAS u32x4*)(L + ob + s * 64);
    const f32x4 wt = *(LAS f32x4*)(L + wa);
    SB();
    f32x4 acc = {0.f, 0.f, 0.f, 0.f};
#pragma unroll
    for (int s = 0; s < 4; ++s) acc = mfma32(af[s], bf[s], acc);
    const float v = fmaf(acc.x, wt.x, acc.y * wt.y) + fmaf(acc.z, wt.z, acc.w * wt.w);
    __builtin_amdgcn_raw_buffer_store_b64(u32x2{pk20(v, tag.x), pk20(dpp<XOR1>(v), tag.y)}, rw, so, 0, AUX_SC1);
}

__global__ void __launch_bounds__(768, 3) fused_decode(const unsigned char* __restrict__ kc, const unsigned char* __restrict__ vc,
                                                       const unsigned char* __restrict__ xin, const unsigned char* __restrict__ wq,
                                                       const unsigned char* __restrict__ w1, const unsigned char* __restrict__ b1,
                                                       const unsigned char* __restrict__ w2, unsigned char* ws, unsigned short* out,
                                                       const unsigned char* __restrict__ wo, const unsigned char* __restrict__ b2) {
    __shared__ __attribute__((aligned(16))) unsigned char smem_[L_END];
    LAS unsigned char* L = (LAS unsigned char*)smem_;
    u64 pc0 = __builtin_amdgcn_s_getpc();
    asm volatile("" : "+s"(pc0));
    int wv = __builtin_amdgcn_readfirstlane(threadIdx.x >> 6);
    const int lane = threadIdx.x & 63;
    const int bi = blockIdx.x;
    const int kvh = 2 * (bi & 7) + (bi >> 7), r = (bi >> 3) & 15;
    const int g = lane >> 4, c = lane & 15, w = wv & 3;
    constexpr float CS = 0.08838834764831845f * 1.4426950408889634f;
#define KV_OFF ((unsigned)((r >> 2) * 16 + kvh) * (SK * 256u) + (SK - WIN) * 256u + (r & 3) * 131072u + w * 1024u)
#define RW_DECL const __amdgpu_buffer_rsrc_t rw = mkrs(ws, WS_END)
#define B0() asm volatile("s_barrier" ::: "memory")

    u32x4 hd[6];
    unsigned warm0 = 0;
    if (wv > 8) {
        __builtin_amdgcn_s_setprio(3);
        const __amdgpu_buffer_rsrc_t rq = mkrs(wq + ((unsigned)((r & 3) * 144 + (w - 1) * 48) * 8192u + (kvh * 256 + (r >> 2) * 64) * 2), 47 * 8192 + 128);
#pragma unroll
        for (int jj = 0; jj < 6; ++jj) hd[jj] = __builtin_amdgcn_raw_buffer_load_b128(rq, (lane >> 3) * 8192 + (lane & 7) * 16, jj * 65536, AUX_NT);
    } else if (wv == 8) {
        __builtin_amdgcn_s_setprio(3);
#pragma unroll
        for (int jj = 0; jj < 5; ++jj) hd[jj] = __builtin_amdgcn_raw_buffer_load_b128(mkrs(xin, 4 * DM * 2), (lane >> 3) * 576 + (lane & 7) * 16 + jj * 128, 0, 0);
    } else if (wv >= 4) {
        *(volatile LAS unsigned*)(L + L_FLAG + lane * 4) = 0u;
        const unsigned ln = (threadIdx.x - 448u) * 4u + ((bi >> 3) & 3u);
        if (ln < CODE_BYTES / 128) warm0 = *(const GAS unsigned*)((const GAS unsigned char*)(pc0 & ~255ull) + ln * 128u);
        if (wv == 7) {
            __builtin_amdgcn_s_setprio(3);
            RW_DECL;
            __builtin_amdgcn_raw_buffer_store_b64(u32x2{SENT, SENT}, rw, WS_O + (kvh * 32 + r) * 256 + (lane >> 5) * 4096 + (lane & 31) * 8, 0, AUX_SC1);
            __builtin_amdgcn_raw_buffer_store_b64(u32x2{SENT, SENT}, rw, lane < 2 ? WS_ML + (kvh * 32 + r) * 8 + lane * 128 : OOB, 0, AUX_SC1);
            hd[0].x = __builtin_amdgcn_raw_buffer_load_b32(rw, WS_EPOCH, 0, AUX_SC1);
        }
        asm volatile("s_waitcnt lgkmcnt(0)" ::: "memory");
    } else {
        __builtin_amdgcn_s_setprio(2);
        const __amdgpu_buffer_rsrc_t rs = mkrs(kc + KV_OFF, 128000);
#pragma unroll
        for (int jj = 0; jj < KHEAD; ++jj) hd[jj] = __builtin_amdgcn_raw_buffer_load_b128(rs, lane * 16, jj * 4096, AUX_NT);
    }
    SB();
    B0();
    asm volatile("" : "+s"(wv));
    kargp kp = (kargp)__builtin_amdgcn_kernarg_segment_ptr();
#define LATE_ARG(T, i) ({ asm volatile("" : "+s"(kp)); (T)kp[i]; })

    if (__builtin_expect(wv < 8, 1)) {
        if (__builtin_expect(wv >= 4, 0)) {
            __builtin_amdgcn_s_setprio(3);
            RW_DECL;
            u32x4 d[32];
            if (w < 3) {
                __builtin_amdgcn_s_sleep(DLY_PARAM);
                const __amdgpu_buffer_rsrc_t rp = mkrs((const void*)((kargp)__builtin_amdgcn_kernarg_segment_ptr())[4 + w], FF * 2);
#pragma unroll
                for (int t = 0; t < 9; ++t) d[t] = __builtin_amdgcn_raw_buffer_load_b128(rp, lane * 16, t * 1024, 0);
#pragma unroll
                for (int t = 0; t < 9; ++t) *(LAS u32x4*)(L + L_PR + w * 9216 + t * 1024 + lane * 16) = d[t];
                lds_signal(L + L_FLAG + F_G0, lane);
            } else {
                const unsigned ep = hd[0].x + 1u;
                *(LAS u32x2*)(L + L_FLAG + F_TAG) = u32x2{ep & 0xfffu, (ep ^ (ep >> 12)) & 0xfffu};
                *(LAS unsigned*)(L + L_FLAG + F_EP) = ep;
                DRAIN();
                asm volatile("" :: "v"(warm0));
                lds_signal(L + L_FLAG + F_WQ, lane);
            }
            __builtin_amdgcn_s_setprio(1);
            const __amdgpu_buffer_rsrc_t rs = mkrs(vc + KV_OFF, 128000);
#pragma unroll
            for (int jj = 0; jj < 32; ++jj) { d[jj] = __builtin_amdgcn_raw_buffer_load_b128(rs, lane * 16, jj * 4096, AUX_NT); SB(); }
            lds_signal(L + L_FLAG + F_VI, lane);
            unsigned sel[2][4];
#pragma unroll
            for (int hs = 0; hs < 2; ++hs) {
                const unsigned bb = (unsigned)(2 * hs) | ((unsigned)(2 * hs + 1) << 8);
                const unsigned on = (c & 1) ? ((bb << 16) | 0x0c0cu) : (0x0c0c0000u | bb);
#pragma unroll
                for (int dd = 0; dd < 4; ++dd) sel[hs][dd] = (((c & 7) >> 1) == dd) ? on : 0x0c0c0c0cu;
            }
            lds_wait<1>(L + L_FLAG + F_P, 4u);
            float wt;
            {
                const f32x4 ma = *(LAS f32x4*)(L + L_ML + (g >> 1) * 32), mb = *(LAS f32x4*)(L + L_ML + (g >> 1) * 32 + 16);
                const float mw = *(LAS float*)(L + L_ML + (g >> 1) * 32 + w * 8);
                wt = __builtin_amdgcn_exp2f(mw - fmaxf(fmaxf(ma.x, ma.z), fmaxf(mb.x, mb.z)));
            }
            int oo = WS_O + ((kvh * 2 + (g >> 1)) * 16 + r) * 256 + c * 16 + (g & 1) * 8;
            asm volatile("" : "+v"(wt), "+v"(oo));
            u32x4 P[4];
#pragma unroll
            for (int t = 0; t < 4; ++t) P[t] = *(LAS u32x4*)(L + L_P + w * 512 + (c >> 3) * 256 + g * 64 + t * 16);
            f32x4 acc = {0.f, 0.f, 0.f, 0.f};
#pragma unroll
            for (int jj = 0; jj < 32; ++jj) {
                const int dw = (jj & 7) * 2 + (jj >> 4), hs = (jj >> 3) & 1;
                const unsigned pv = P[dw >> 2][dw & 3];
                u32x4 a;
#pragma unroll
                for (int dd = 0; dd < 4; ++dd) a[dd] = __builtin_amdgcn_perm(pv, pv, sel[hs][dd]);
                acc = mfma32(a, d[jj], acc);
            }
            acc *= wt;
            *(LAS f32x4*)(L + L_H + w * 1024 + lane * 16) = acc;
            WG_BARRIER();
            if (w == 0) {
                acc += *(LAS f32x4*)(L + L_H + 1024 + lane * 16);
                acc += *(LAS f32x4*)(L + L_H + 2048 + lane * 16) + *(LAS f32x4*)(L + L_H + 3072 + lane * 16);
                u32x2 o; o.x = min(pk_bf16(acc.x, acc.y), SENT - 1); o.y = min(pk_bf16(acc.z, acc.w), SENT - 1);
                __builtin_amdgcn_raw_buffer_store_b64(o, rw, oo, 0, AUX_SC1);
                if ((g >> 1) == (r >> 3)) *(LAS u32x2*)(L + L_O + r * 272 + c * 16 + (g & 1) * 8) = o;
                asm volatile("s_waitcnt lgkmcnt(0)" ::: "memory");
            }
        } else {
            u32x4 d[32];
            const __amdgpu_buffer_rsrc_t rs = mkrs(kc + KV_OFF, 128000);
#pragma unroll
            for (int jj = 0; jj < KHEAD; ++jj) d[jj] = hd[jj];
#pragma unroll
            for (int jj = KHEAD; jj < 32; ++jj) { d[jj] = __builtin_amdgcn_raw_buffer_load_b128(rs, lane * 16, jj * 4096, AUX_NT); SB(); }
            RW_DECL;
            lds_wait<2>(L + L_FLAG + F_Q, 1u);
            const u32x4 qown = *(LAS u32x4*)(L + L_Q + (c >> 3) * 256 + c * 16);
            const u32x4 qoth = *(LAS u32x4*)(L + L_Q + ((c >> 3) ^ 1) * 256 + c * 16);
            float s[4];
#pragma unroll
            for (int q8 = 0; q8 < 4; ++q8) {
                float y[8];
#pragma unroll
                for (int t = 0; t < 8; ++t) {
                    const u32x4 kk = d[q8 * 8 + t];
                    float xo = 0.f, xt = 0.f;
#pragma unroll
                    for (int e = 0; e < 4; ++e) { xo = dot2(kk[e], qown[e], xo); xt = dot2(kk[e], qoth[e], xt); }
                    y[t] = xo + dpp<ROR8>(xt);
                }
                float z[4], u[2];
#pragma unroll
                for (int t = 0; t < 4; ++t) { const float a = y[t] + dpp<MIR8>(y[t]), b = y[t + 4] + dpp<MIR8>(y[t + 4]); z[t] = (c & 4) ? b : a; }
#pragma unroll
                for (int t = 0; t < 2; ++t) { const float a = z[t] + dpp<XOR2>(z[t]), b = z[t + 2] + dpp<XOR2>(z[t + 2]); u[t] = (c & 2) ? b : a; }
                { const float a = u[0] + dpp<XOR1>(u[0]), b = u[1] + dpp<XOR1>(u[1]); s[q8] = ((c & 1) ? b : a) * CS; }
            }
            float m = fmaxf(fmaxf(s[0], s[1]), fmaxf(s[2], s[3]));
            m = fmaxf(m, dpp<XOR1>(m)); m = fmaxf(m, dpp<XOR2>(m)); m = fmaxf(m, dpp<MIR8>(m));
            m = xrow_max(m);
            u32x2 pp;
            pp.x = pk_bf16(__builtin_amdgcn_exp2f(s[0] - m), __builtin_amdgcn_exp2f(s[1] - m));
            pp.y = pk_bf16(__builtin_amdgcn_exp2f(s[2] - m), __builtin_amdgcn_exp2f(s[3] - m));
            *(LAS u32x2*)(L + L_P + w * 512 + (c >> 3) * 256 + g * 64 + (c & 7) * 8) = pp;
            float l = (lo_f(pp.x) + hi_f(pp.x)) + (lo_f(pp.y) + hi_f(pp.y));
            l += dpp<XOR1>(l); l += dpp<XOR2>(l); l += dpp<MIR8>(l);
            l = xrow_sum(l);
            if ((lane & 55) == 0) *(LAS f32x2*)(L + L_ML + (c >> 3) * 32 + w * 8) = f32x2{m, l};
            lds_signal(L + L_FLAG + F_P, lane);
            u32x4 wr[5];
            const __amdgpu_buffer_rsrc_t ro = mkrs(LATE_ARG(const unsigned char*, 9) + (size_t)(kvh * 256 + (r >> 3) * 128) * 1152 + (r & 7) * 144, 127 * 1152 + 144);
            int wof[5];
#pragma unroll
            for (int t = 0; t < 5; ++t) { const int ch = (t * 4 + w) * 64 + lane; wof[t] = (ch / 9) * 1152 + (ch % 9) * 16; asm volatile("" : "+v"(wof[t])); }
            int so = (c & 1) == 0 ? WS_S + (((kvh * 2 + (r >> 3)) * 8 + (r & 7)) * 4 + g) * 320 + w * 64 + c * 4 : OOB;
            u32x2 tag = *(LAS u32x2*)(L + L_FLAG + F_TAG);
            asm volatile("" : "+v"(so), "+v"(tag));
            lds_wait<1>(L + L_FLAG + F_VI, 4u);
#pragma unroll
            for (int t = 0; t < 5; ++t) wr[t] = __builtin_amdgcn_raw_buffer_load_b128(ro, wof[t], 0, AUX_NT);
            SB();
            __builtin_amdgcn_s_barrier();
#pragma unroll
            for (int t = 0; t < 5; ++t) *(LAS u32x4*)(L + L_WO + (t * 4 + w) * 1024 + lane * 16) = wr[t];
            lds_signal(L + L_FLAG + F_WO, lane);
            out_tile(L, rw, w, g, c, so, tag);
        }
    } else if (wv == 8) {
        const int voff = (lane >> 3) * 576 + (lane & 7) * 16;
        RW_DECL;
        {
            float xv[40];
            float sum = 0.f;
#pragma unroll
            for (int t = 0; t < 5; ++t)
#pragma unroll
                for (int e = 0; e < 4; ++e) { const unsigned z = (t < 4 || (c & 7) < 4) ? hd[t][e] : 0u; xv[8 * t + 2 * e] = lo_f(z); xv[8 * t + 2 * e + 1] = hi_f(z); }
#pragma unroll
            for (int t = 0; t < 40; ++t) sum += xv[t];
            const float mean = row_sum(sum) * (1.0f / DM);
            const float mean4 = (c & 7) < 4 ? mean : 0.f;
            float var = 0.f;
#pragma unroll
            for (int t = 0; t < 40; ++t) { xv[t] -= (t < 32 ? mean : mean4); var = fmaf(xv[t], xv[t], var); }
            const float rstd = __builtin_amdgcn_rsqf(row_sum(var) * (1.0f / DM) + 1e-5f);
#pragma unroll
            for (int t = 0; t < 5; ++t) {
                u32x4 o;
#pragma unroll
                for (int e = 0; e < 4; ++e) o[e] = pk_bf16(xv[8 * t + 2 * e] * rstd, xv[8 * t + 2 * e + 1] * rstd);
                if (t < 4 || (c & 7) < 4) *(LAS u32x4*)(L + L_XN + voff + t * 128) = o;
            }
            lds_signal(L + L_FLAG + F_G0, lane);
        }
        lds_wait<1>(L + L_FLAG + F_WQ, 4u);
        const u32x2 tag = *(LAS u32x2*)(L + L_FLAG + F_TAG);
        {
            LAS unsigned char* xa = L + L_XN + (c & 3) * 1152 + (r & 3) * 288;
            LAS unsigned char* wb = L + L_WQ + (c >> 2) * 128 + (c & 3) * 8;
            f32x4 acc[4];
#pragma unroll
            for (int t = 0; t < 4; ++t) acc[t] = f32x4{0.f, 0.f, 0.f, 0.f};
#pragma unroll
            for (int s = 0; s < 4; ++s) {
                const u32x4 a = *(LAS u32x4*)(xa + s * 64 + g * 16);
#pragma unroll
                for (int t = 0; t < 4; ++t) {
                    const u32x2 b0 = tr_read(wb + (s * 32 + g * 8) * 128 + t * 32), b1_ = tr_read(wb + (s * 32 + g * 8 + 4) * 128 + t * 32);
                    acc[t] = mfma32(a, u32x4{b0.x, b0.y, b1_.x, b1_.y}, acc[t]);
                }
            }
            const u32x2 a4 = *(LAS u32x2*)(xa + 256 + g * 8);
#pragma unroll
            for (int t = 0; t < 4; ++t) acc[t] = mfma16(a4, tr_read(wb + (128 + g * 4) * 128 + t * 32), acc[t]);
            float pv[4];
#pragma unroll
            for (int t = 0; t < 4; ++t) pv[t] = g == 0 ? acc[t].x : (g == 1 ? acc[t].y : (g == 2 ? acc[t].z : acc[t].w));
            const int qo = WS_Q + ((kvh * 4 + g) * 4 + (r & 3)) * 1024 + (r >> 2) * 256 + c * 16;
            __builtin_amdgcn_raw_buffer_store_b128(u32x4{pk20(pv[0], tag.x), pk20(pv[1], tag.y), pk20(pv[2], tag.x), pk20(pv[3], tag.y)}, rw, qo, 0, AUX_SC1);
        }
        __builtin_amdgcn_s_sleep(DLY_Q);
        {
            u32x4 gq[4];
            const int qi = WS_Q + (kvh * 4 + (r >> 2)) * 4096 + lane * 16;
#pragma unroll 1
            for (unsigned spins = 0; spins < SPIN_CAP; ++spins) {
                MEMBAR();
                unsigned bad = 0;
#pragma unroll
                for (int t = 0; t < 4; ++t) gq[t] = __builtin_amdgcn_raw_buffer_load_b128(rw, qi, t * 1024, AUX_SC1);
#pragma unroll
                for (int t = 0; t < 4; ++t) bad |= bad20(gq[t], tag);
                if (__all((bad & 0xfffu) == 0u)) break;
                __builtin_amdgcn_s_sleep(8);
            }
#pragma unroll
            for (int t = 0; t < 4; ++t)
                *(LAS unsigned short*)(L + L_Q + (g * 64 + t * 16 + c) * 2) = (unsigned short)pk_bf16((un20(gq[0][t]) + un20(gq[1][t])) + (un20(gq[2][t]) + un20(gq[3][t])), 0.f);
            lds_signal(L + L_FLAG + F_Q, lane);
        }
        lds_wait<4>(L + L_FLAG + F_P, 4u);
        {
            const f32x4 a = *(LAS f32x4*)(L + L_ML + (lane & 1) * 32), b = *(LAS f32x4*)(L + L_ML + (lane & 1) * 32 + 16);
            const float M = fmaxf(fmaxf(a.x, a.z), fmaxf(b.x, b.z));
            const float Ls = __builtin_amdgcn_exp2f(a.x - M) * a.y + __builtin_amdgcn_exp2f(a.z - M) * a.w + __builtin_amdgcn_exp2f(b.x - M) * b.y +
                             __builtin_amdgcn_exp2f(b.z - M) * b.w;
            u32x2 o; o.x = min(__float_as_uint(M), SENT - 1); o.y = min(__float_as_uint(Ls), SENT - 1);
            __builtin_amdgcn_raw_buffer_store_b64(o, rw, lane < 2 ? WS_ML + ((kvh * 2 + lane) * 16 + r) * 8 : OOB, 0, AUX_SC1);
        }
        int oi[4], ol[4];
#pragma unroll
        for (int t = 0; t < 4; ++t) {
            const bool own = t * 4 + g == r;
            oi[t] = own ? OOB : WS_O + (kvh * 2 + (r >> 3)) * 4096 + t * 1024 + lane * 16;
            ol[t] = own ? L_H : L_O + (t * 4 + g) * 272 + c * 16;
            asm volatile("" : "+v"(oi[t]), "+v"(ol[t]));
        }
        __builtin_amdgcn_s_barrier();
        __builtin_amdgcn_s_sleep(DLY_O);
        {
            u32x4 ov[4];
#pragma unroll 1
            for (unsigned spins = 0; spins < SPIN_CAP; ++spins) {
                MEMBAR();
#pragma unroll
                for (int t = 0; t < 4; ++t) ov[t] = __builtin_amdgcn_raw_buffer_load_b128(rw, oi[t], 0, AUX_SC1);
                unsigned mx = 0;
#pragma unroll
                for (int t = 0; t < 4; ++t) { mx = max(mx, max(max(ov[t].x, ov[t].y), max(ov[t].z, ov[t].w))); *(LAS u32x4*)(L + ol[t]) = ov[t]; }
                if (__all(mx != SENT)) break;
            }
        }
        WG_BARRIER();
    } else {
        const int hw = wv - 9;
        RW_DECL;
        const int ci = (bi & 7) * 32 + (bi >> 3);
#pragma unroll
        for (int t = 0; t < 6; ++t) *(LAS u32x4*)(L + L_WQ + (hw * 6 + t) * 1024 + lane * 16) = hd[t];
        lds_signal(L + L_FLAG + F_WQ, lane);
        const int cq = lane & 3, oj = cq * 4 + (((g & 1) << 1) | (g >> 1)) - ((ci * 9) & 3);
        const bool ov = (lane & 12) == 0 && (unsigned)oj < 9u && cq < 3;
        const int oi = ov ? oj : 0;
        __builtin_amdgcn_s_setprio(0);
        lds_wait<2>(L + L_FLAG + F_G0, 4u);
        const int xl = lane < 36 ? lane : 0;
        const u32x4 xa = *(LAS u32x4*)(L + L_XN + (ci >> 6) * 1152 + xl * 32), xb = *(LAS u32x4*)(L + L_XN + (ci >> 6) * 1152 + xl * 32 + 16);
        const float msk = lane < 36 ? 1.0f : 0.0f;
        float xv[16];
#pragma unroll
        for (int e = 0; e < 4; ++e) { xv[2 * e] = lo_f(xa[e]); xv[2 * e + 1] = hi_f(xa[e]); xv[8 + 2 * e] = lo_f(xb[e]); xv[8 + 2 * e + 1] = hi_f(xb[e]); }
        float sum = 0.f;
#pragma unroll
        for (int e = 0; e < 16; ++e) sum += xv[e];
        const float mean = xrow_sum(row_sum(sum * msk)) * (1.0f / DM);
        float var = 0.f;
#pragma unroll
        for (int e = 0; e < 16; ++e) { const float dd = xv[e] - mean; var = fmaf(dd, dd, var); }
        const float rstd = __builtin_amdgcn_rsqf(xrow_sum(row_sum(var * msk)) * (1.0f / DM) + 1e-5f);
        f32x2 vw1[12], vb1[12], vw2[12];
#pragma unroll
        for (int t = 0; t < 3; ++t) {
            const u32x4 r1 = *(LAS u32x4*)(L + L_PR + (hw * 3 + t) * 1024 + lane * 16), rb = *(LAS u32x4*)(L + L_PR + 9216 + (hw * 3 + t) * 1024 + lane * 16),
                        r2 = *(LAS u32x4*)(L + L_PR + 18432 + (hw * 3 + t) * 1024 + lane * 16);
#pragma unroll
            for (int e = 0; e < 4; ++e) {
                vw1[t * 4 + e] = f32x2{lo_f(r1[e]), hi_f(r1[e])};
                vb1[t * 4 + e] = f32x2{lo_f(rb[e]), hi_f(rb[e])};
                vw2[t * 4 + e] = f32x2{lo_f(r2[e]), hi_f(r2[e])};
            }
        }
        constexpr float C1 = -2.0f * 0.7978845608028654f * 1.4426950408889634f, C2 = C1 * 0.044715f;
        LAS unsigned short* xs = (LAS unsigned short*)(L + L_XN + ci * 18);
        unsigned xn_next = xs[0];
        float ysum = 0.f, prev = 0.f;
#pragma unroll 1
        for (int o = 0; o < 9; ++o) {
            const float h = lo_f(pk_bf16((lo_f(xn_next) - mean) * rstd, 0.f));
            xn_next = xs[o + 1];
            f32x2 u[12], a[12];
#pragma unroll
            for (int t = 0; t < 12; ++t) u[t] = vw1[t] * h + vb1[t];
#pragma unroll
            for (int t = 0; t < 12; ++t) a[t] = u[t] * u[t];
            const float tot = xrow_sum(row_sum(prev));
            ysum = lane + 1 == o ? tot : ysum;
#pragma unroll
            for (int t = 0; t < 12; ++t) a[t] = a[t] * C2 + C1;
#pragma unroll
            for (int t = 0; t < 12; ++t) a[t] = a[t] * u[t];
            SB();
#pragma unroll
            for (int t = 0; t < 12; ++t) { a[t].x = __builtin_amdgcn_exp2f(a[t].x); a[t].y = __builtin_amdgcn_exp2f(a[t].y); u[t] = u[t] * vw2[t]; }
            SB();
#pragma unroll
            for (int t = 0; t < 12; ++t) a[t] += 1.0f;
            SB();
#pragma unroll
            for (int t = 0; t < 12; ++t) { a[t].x = __builtin_amdgcn_rcpf(a[t].x); a[t].y = __builtin_amdgcn_rcpf(a[t].y); }
            SB();
            f32x2 s0 = u[0] * a[0], s1 = u[1] * a[1], s2 = u[2] * a[2];
#pragma unroll
            for (int t = 3; t < 12; t += 3) { s0 += u[t] * a[t]; s1 += u[t + 1] * a[t + 1]; s2 += u[t + 2] * a[t + 2]; }
            s0 += s1 + s2;
            prev = s0.x + s0.y;
        }
        { const float tot = xrow_sum(row_sum(prev)); ysum = lane == 8 ? tot : ysum; }
        if (lane < 9) *(LAS float*)(L + L_Y + hw * 64 + lane * 4) = ysum;
        __builtin_amdgcn_s_setprio(3);
        unsigned xraw = 0, braw = 0;
        if (hw == 0) { xraw = *(const GAS unsigned short*)(xin + (ci * 9 + oi) * 2); braw = *LATE_ARG(const GAS unsigned short*, 10); }
        WG_BARRIER();
        const u32x2 tag = *(LAS u32x2*)(L + L_FLAG + F_TAG);
        if (hw == 0) {
            const float y = *(LAS float*)(L + L_Y + oi * 4) + *(LAS float*)(L + L_Y + 64 + oi * 4) + *(LAS float*)(L + L_Y + 128 + oi * 4) + lo_f(braw);
            float gt = y * __builtin_amdgcn_rcpf(1.0f + __builtin_amdgcn_exp2f(-1.4426950408889634f * y));
            float xr = lo_f(xraw);
            int si = cq < 3 ? WS_S + (((lane >> 2) * 8 + ((ci >> 3) & 7)) * 4 + (ci >> 6)) * 320 + ((((ci & 7) * 9) >> 2) + cq) * 16 : OOB;
            int oo = ov ? (ci * 9 + oi) * 2 : OOB;
            const __amdgpu_buffer_rsrc_t rout = mkrs(LATE_ARG(const void*, 8), 4 * DM * 2);
            unsigned want = cq < 3 ? 0xfffu : 0u;
            const unsigned ep = *(LAS unsigned*)(L + L_FLAG + F_EP);
            asm volatile("" : "+v"(xr), "+v"(gt), "+v"(si), "+v"(oo), "+v"(want));
            __builtin_amdgcn_s_barrier();
            __builtin_amdgcn_s_sleep(DLY_S);
            u32x4 sa, sb;
#pragma unroll 1
            for (unsigned spins = 0; spins < SPIN_CAP; ++spins) {
                MEMBAR();
                sa = __builtin_amdgcn_raw_buffer_load_b128(rw, si, 0, AUX_SC1);
                sb = __builtin_amdgcn_raw_buffer_load_b128(rw, si, 16 * 32 * 320, AUX_SC1);
                if (__all(((bad20(sa, tag) | bad20(sb, tag)) & want) == 0u)) break;
            }
            float s = swap16_sum(swap32_sum(un20(sa.x) + un20(sb.x), un20(sa.y) + un20(sb.y)), swap32_sum(un20(sa.z) + un20(sb.z), un20(sa.w) + un20(sb.w)));
            s += dpp<ROR8>(s);
            s += dpp<ROR4>(s);
            __builtin_amdgcn_raw_buffer_store_b16((unsigned short)pk_bf16(fmaf(s, gt, xr), 0.f), rout, oo, 0, 0);
            if (bi == 0)
                __builtin_amdgcn_raw_buffer_store_b32(ep, rw, lane == 0 ? WS_EPOCH : OOB, 0, AUX_SC1);
        } else if (hw == 1) {
            const int so = (c & 9) == 0 ? WS_S + (((kvh * 2 + (r >> 3)) * 8 + (r & 7)) * 4 + g) * 320 + 256 + c * 4 : OOB;
            out_tile(L, rw, 4, g, c, so, tag);
        } else {
            const int mi = WS_ML + ((kvh * 2 + (r >> 3)) * 16 + c) * 8;
            u32x2 ml;
            __builtin_amdgcn_s_sleep(DLY_ML);
#pragma unroll 1
            for (unsigned spins = 0; spins < SPIN_CAP; ++spins) {
                MEMBAR();
                ml = __builtin_amdgcn_raw_buffer_load_b64(rw, mi, 0, AUX_SC1);
                if (__all(max(ml.x, ml.y) != SENT)) break;
                __builtin_amdgcn_s_sleep(2);
            }
            const float mp = __uint_as_float(ml.x);
            float M = fmaxf(mp, dpp<XOR1>(mp)); M = fmaxf(M, dpp<XOR2>(M));
            const float ew = __builtin_amdgcn_exp2f(mp - M);
            float Ls = ew * __uint_as_float(ml.y); Ls += dpp<XOR1>(Ls); Ls += dpp<XOR2>(Ls);
            *(LAS float*)(L + L_WT + c * 4) = ew * __builtin_amdgcn_rcpf(Ls);
            asm volatile("s_waitcnt lgkmcnt(0)" ::: "memory");
        }
    }
}

extern "C" void kernel_launch(void* const* d_in, const int* in_sizes, int n_in, void* d_out, int out_size, void* d_ws, size_t ws_size,
                              hipStream_t stream) {
    typedef const unsigned char* P;
    fused_decode<<<256, 768, 0, stream>>>((P)d_in[1], (P)d_in[2], (P)d_in[0], (P)d_in[3], (P)d_in[6], (P)d_in[7], (P)d_in[8], (unsigned char*)d_ws,
                                          (unsigned short*)d_out, (P)d_in[4], (P)d_in[9]);
}
```

```cpp
#include <hip/hip_runtime.h>
#include <hip/hip_bf16.h>
#include <stdint.h>

typedef unsigned long long u64;
typedef unsigned u32x4 __attribute__((ext_vector_type(4)));
typedef unsigned u32x2 __attribute__((ext_vector_type(2)));
typedef float f32x4 __attribute__((ext_vector_type(4)));
typedef float f32x2 __attribute__((ext_vector_type(2)));
typedef short s16x4 __attribute__((ext_vector_type(4)));
typedef __bf16 bf16x2 __attribute__((ext_vector_type(2)));
typedef __bf16 bf16x8 __attribute__((ext_vector_type(8)));
#define LAS __attribute__((address_space(3)))
#define GAS __attribute__((address_space(1)))
typedef __attribute__((address_space(4))) const u64* kargp;
#define SB() __builtin_amdgcn_sched_barrier(0)

constexpr int DM = 576, SK = 8192, WIN = 2048, FF = 4608;
constexpr int AUX_NT = 2, AUX_SC1 = 16;
constexpr unsigned SENT = 0xFFFFFFFFu;
constexpr int OOB = 0x7fffff00;
constexpr unsigned SPIN_CAP = 1u << 20;
constexpr int CODE_BYTES = 19712;
constexpr int DLY_Q = 44, DLY_O = 18, DLY_ML = 8, DLY_S = 18;
constexpr int TAU = 15, T0_K = 22, T0_V = 497, T_END = 940, T_RD = 270, CATCH = 60;
constexpr int PV0 = 22, PVN = 3;
constexpr int KHEAD = 3;
constexpr int WS_EPOCH = 0;
constexpr int WS_T = 512;
constexpr int WS_ML = 1024;
constexpr int WS_Q = 8192;
constexpr int WS_O = WS_Q + 16 * 4 * 4 * 1024;
constexpr int WS_S = WS_O + 16 * 2 * 16 * 256;
constexpr int WS_END = WS_S + 1024 * 320;
constexpr int L_FLAG = 0, L_Q = 256, L_P = L_Q + 512, L_ML = L_P + 2048, L_Y = L_ML + 64, L_WT = L_Y + 192, L_O = L_WT + 64, L_XN = L_O + 4352,
              L_WQ = L_XN + 4624, L_WO = L_WQ + 18432, L_H = L_WO + 20480, L_PR = L_H + 4096, L_END = L_PR + 3 * 9216;
constexpr int F_Q = 0, F_WQ = 4, F_TAG = 8, F_G0 = 16, F_P = 20, F_WO = 24, F_VI = 28, F_EP = 32, F_TV = 36, F_TD = 40;

__device__ __forceinline__ __amdgpu_buffer_rsrc_t mkrs(const void* p, int bytes) { return __builtin_amdgcn_make_buffer_rsrc((void*)p, 0, bytes, 0x00020000); }
__device__ __forceinline__ float lo_f(unsigned v) { return __builtin_bit_cast(float, v << 16); }
__device__ __forceinline__ float hi_f(unsigned v) { return __builtin_bit_cast(float, v & 0xffff0000u); }
__device__ __forceinline__ unsigned pk_bf16(float lo, float hi) {
    f32x2 t = {lo, hi};
    return __builtin_bit_cast(unsigned, __builtin_convertvector(t, bf16x2));
}
__device__ __forceinline__ float rbf(float v) { return lo_f(pk_bf16(v, 0.f)); }
__device__ __forceinline__ unsigned pk20(float v, unsigned t) { return ((__float_as_uint(v) + 0x800u) & 0xfffff000u) | t; }
__device__ __forceinline__ float un20(unsigned w) { return __uint_as_float(w & 0xfffff000u); }
__device__ __forceinline__ unsigned bad20(u32x4 w, u32x2 t) { return (w.x ^ t.x) | (w.y ^ t.y) | (w.z ^ t.x) | (w.w ^ t.y); }
__device__ __forceinline__ float dot2(unsigned a, unsigned b, float c) {
    return __builtin_amdgcn_fdot2_f32_bf16(__builtin_bit_cast(bf16x2, a), __builtin_bit_cast(bf16x2, b), c, false);
}
template <int CTRL> __device__ __forceinline__ float dpp(float x) {
    return __builtin_bit_cast(float, __builtin_amdgcn_mov_dpp(__builtin_bit_cast(int, x), CTRL, 0xf, 0xf, true));
}
constexpr int XOR1 = 0xB1, XOR2 = 0x4E, MIR8 = 0x141, ROR8 = 0x128, ROR4 = 0x124;
__device__ __forceinline__ float row_sum(float x) { x += dpp<XOR1>(x); x += dpp<XOR2>(x); x += dpp<MIR8>(x); x += dpp<ROR8>(x); return x; }
__device__ __forceinline__ float swap32_sum(float a, float b) {
    auto s = __builtin_amdgcn_permlane32_swap(__float_as_uint(a), __float_as_uint(b), false, false);
    return __uint_as_float(s[0]) + __uint_as_float(s[1]);
}
__device__ __forceinline__ float swap16_sum(float a, float b) {
    auto s = __builtin_amdgcn_permlane16_swap(__float_as_uint(a), __float_as_uint(b), false, false);
    return __uint_as_float(s[0]) + __uint_as_float(s[1]);
}
__device__ __forceinline__ float xrow_sum(float x) { x = swap16_sum(x, x); return swap32_sum(x, x); }
__device__ __forceinline__ float xrow_max(float x) {
    auto s = __builtin_amdgcn_permlane16_swap(__float_as_uint(x), __float_as_uint(x), false, false);
    x = fmaxf(__uint_as_float(s[0]), __uint_as_float(s[1]));
    auto t = __builtin_amdgcn_permlane32_swap(__float_as_uint(x), __float_as_uint(x), false, false);
    return fmaxf(__uint_as_float(t[0]), __uint_as_float(t[1]));
}
__device__ __forceinline__ f32x4 mfma32(u32x4 a, u32x4 b, f32x4 c) {
    return __builtin_amdgcn_mfma_f32_16x16x32_bf16(__builtin_bit_cast(bf16x8, a), __builtin_bit_cast(bf16x8, b), c, 0, 0, 0);
}
__device__ __forceinline__ f32x4 mfma16(u32x2 a, u32x2 b, f32x4 c) {
    return __builtin_amdgcn_mfma_f32_16x16x16bf16_1k(__builtin_bit_cast(s16x4, a), __builtin_bit_cast(s16x4, b), c, 0, 0, 0);
}
__device__ __forceinline__ u32x2 tr_read(LAS unsigned char* p) {
    return __builtin_bit_cast(u32x2, __builtin_amdgcn_ds_read_tr16_b64_v4i16((LAS s16x4*)p));
}
template <int SLP> __device__ __forceinline__ void lds_wait(LAS unsigned char* p, unsigned want) {
#pragma unroll 1
    for (unsigned spins = 0; spins < SPIN_CAP * 64; ++spins) {
        if ((unsigned)__builtin_amdgcn_readfirstlane(*(volatile LAS unsigned*)p) >= want) break;
        if (SLP) __builtin_amdgcn_s_sleep(SLP);
    }
    asm volatile("" ::: "memory");
}
__device__ __forceinline__ void lds_signal(LAS unsigned char* p, int lane) {
    asm volatile("s_waitcnt lgkmcnt(0)" ::: "memory");
    if (lane == 0) asm volatile("ds_add_u32 %0, %1" :: "v"((unsigned)(size_t)p), "v"(1u) : "memory");
}
__device__ __forceinline__ void pace(unsigned& dl) {
    asm volatile("1:\n\ts_memrealtime vcc\n\ts_waitcnt lgkmcnt(0)\n\ts_sub_u32 vcc_lo, vcc_lo, %0\n\ts_cmp_lt_i32 vcc_lo, 0\n\ts_cbranch_scc1 1b\n\ts_add_u32 %0, %0, %1"
                 : "+s"(dl) : "i"(TAU) : "vcc", "scc", "memory");
}
__device__ __forceinline__ void pace8(unsigned& dl, unsigned tau) {
    asm volatile("1:\n\ts_memrealtime vcc\n\ts_waitcnt lgkmcnt(0)\n\ts_lshl_b32 vcc_lo, vcc_lo, 3\n\ts_sub_u32 vcc_lo, vcc_lo, %0\n\ts_cmp_lt_i32 vcc_lo, 0\n\ts_cbranch_scc1 1b\n\ts_add_u32 %0, %0, %1"
                 : "+s"(dl) : "s"(tau) : "vcc", "scc", "memory");
}
__device__ __forceinline__ void sleep_until(unsigned t) {
#pragma unroll 1
    for (int i = 0; i < 4096 && (int)((unsigned)__builtin_amdgcn_s_memrealtime() - t) < 0; ++i) __builtin_amdgcn_s_sleep(4);
}
#define MEMBAR() asm volatile("" ::: "memory")
#define WG_BARRIER() asm volatile("s_waitcnt lgkmcnt(0)\n\ts_barrier" ::: "memory")
#define DRAIN() asm volatile("s_waitcnt vmcnt(0)" ::: "memory")

__device__ __forceinline__ void out_tile(LAS unsigned char* L, __amdgpu_buffer_rsrc_t rw, int t5, int g, int c, int so, u32x2 tag) {
    LAS unsigned char* wb = L + L_WO + (g * 8 + (c >> 2)) * 144 + (c & 3) * 8 + t5 * 32;
    unsigned ob = L_O + c * 272 + g * 16, wa = L_WT + g * 16;
    lds_wait<0>(L + L_FLAG + F_WO, 4u);
    u32x4 bf[4], af[4];
#pragma unroll
    for (int s = 0; s < 4; ++s) {
        const u32x2 b0 = tr_read(wb + s * 32 * 144), b1 = tr_read(wb + (s * 32 + 4) * 144);
        bf[s] = u32x4{b0.x, b0.y, b1.x, b1.y};
    }
    asm volatile("" : "+v"(bf[0]), "+v"(bf[1]), "+v"(bf[2]), "+v"(bf[3]), "+v"(ob), "+v"(wa));
    WG_BARRIER();
#pragma unroll
    for (int s = 0; s < 4; ++s) af[s] = *(LAS u32x4*)(L + ob + s * 64);
    const f32x4 wt = *(LAS f32x4*)(L + wa);
    SB();
    f32x4 acc = {0.f, 0.f, 0.f, 0.f};
#pragma unroll
    for (int s = 0; s < 4; ++s) acc = mfma32(af[s], bf[s], acc);
    const float v = fmaf(acc.x, wt.x, acc.y * wt.y) + fmaf(acc.z, wt.z, acc.w * wt.w);
    __builtin_amdgcn_raw_buffer_store_b64(u32x2{pk20(v, tag.x), pk20(dpp<XOR1>(v), tag.y)}, rw, so, 0, AUX_SC1);
}

__global__ void __launch_bounds__(768, 3) fused_decode(const unsigned char* __restrict__ kc, const unsigned char* __restrict__ vc,
                                                       const unsigned char* __restrict__ xin, const unsigned char* __restrict__ wq,
                                                       const unsigned char* __restrict__ w1, const unsigned char* __restrict__ b1,
                                                       const unsigned char* __restrict__ w2, unsigned char* ws, unsigned short* out,
                                                       const unsigned char* __restrict__ wo, const unsigned char* __restrict__ b2) {
    __shared__ __attribute__((aligned(16))) unsigned char smem_[L_END];
    LAS unsigned char* L = (LAS unsigned char*)smem_;
    u64 pc0 = __builtin_amdgcn_s_getpc();
    asm volatile("" : "+s"(pc0));
    int wv = __builtin_amdgcn_readfirstlane(threadIdx.x >> 6);
    const int lane = threadIdx.x & 63;
    const int bi = blockIdx.x;
    const int kvh = 2 * (bi & 7) + ((bi >> 3) & 1), r = ((bi >> 2) & 12) + (bi >> 6);
    const int g = lane >> 4, c = lane & 15, w = wv & 3;
#define KV_OFF ((unsigned)((r >> 2) * 16 + kvh) * (SK * 256u) + (SK - WIN) * 256u + (r & 3) * 16384u + w * 256u)
#define KV_LANE (g * 4096 + c * 16)
#define KV_SPAN (7 * 65536 + 3 * 4096 + 3 * 1024 + 256)
#define RW_DECL const __amdgpu_buffer_rsrc_t rw = mkrs(ws, WS_END)
#define B0() asm volatile("s_barrier" ::: "memory")

    u32x4 hd[6];
    if (wv > 8) {
        __builtin_amdgcn_s_setprio(3);
        const __amdgpu_buffer_rsrc_t rq = mkrs(wq + ((unsigned)((r & 3) * 144 + (w - 1) * 48) * 8192u + (kvh * 256 + (r >> 2) * 64) * 2), 47 * 8192 + 128);
#pragma unroll
        for (int jj = 0; jj < 6; ++jj) hd[jj] = __builtin_amdgcn_raw_buffer_load_b128(rq, (lane >> 3) * 8192 + (lane & 7) * 16, jj * 65536, AUX_NT);
    } else if (wv == 8) {
        __builtin_amdgcn_s_setprio(3);
#pragma unroll
        for (int jj = 0; jj < 5; ++jj) hd[jj] = __builtin_amdgcn_raw_buffer_load_b128(mkrs(xin, 4 * DM * 2), (lane >> 3) * 576 + (lane & 7) * 16 + jj * 128, 0, 0);
    } else if (wv >= 4) {
        __builtin_amdgcn_s_setprio(3);
        if (wv != 7) {
            const kargp ka = (kargp)__builtin_amdgcn_kernarg_segment_ptr();
            hd[0].x = __builtin_amdgcn_raw_buffer_load_b32(mkrs((const void*)(pc0 & ~255ull), CODE_BYTES), ((threadIdx.x - 256u) * 4u + ((bi >> 3) & 3u)) * 128u, 0, 0);
            hd[0].y = *(const GAS unsigned short*)ka[4 + w];
            hd[0].z = *(const GAS unsigned short*)(ka[8 + w] + (w == 0 ? ((bi & 7) * 32 + (bi >> 3)) * 18 : 0));
        } else {
            *(volatile LAS unsigned*)(L + L_FLAG + lane * 4) = 0u;
            RW_DECL;
            __builtin_amdgcn_raw_buffer_store_b64(u32x2{SENT, SENT}, rw, WS_O + (kvh * 32 + r) * 256 + (lane >> 5) * 4096 + (lane & 31) * 8, 0, AUX_SC1);
            __builtin_amdgcn_raw_buffer_store_b64(u32x2{SENT, SENT}, rw, lane < 2 ? WS_ML + (kvh * 32 + r) * 8 + lane * 128 : OOB, 0, AUX_SC1);
            hd[0].x = __builtin_amdgcn_raw_buffer_load_b32(rw, WS_EPOCH, 0, AUX_SC1);
            asm volatile("s_waitcnt lgkmcnt(0)" ::: "memory");
        }
    } else {
        __builtin_amdgcn_s_setprio(2);
        const __amdgpu_buffer_rsrc_t rs = mkrs(kc + KV_OFF, KV_SPAN);
#pragma unroll
        for (int jj = 0; jj < KHEAD; ++jj) hd[jj] = __builtin_amdgcn_raw_buffer_load_b128(rs, KV_LANE + (jj & 3) * 1024, (jj >> 2) * 65536, AUX_NT);
    }
    SB();
    B0();
    const unsigned tstart = (unsigned)__builtin_amdgcn_s_memrealtime();
    asm volatile("" : "+s"(wv));
    kargp kp = (kargp)__builtin_amdgcn_kernarg_segment_ptr();
#define LATE_ARG(T, i) ({ asm volatile("" : "+s"(kp)); (T)kp[i]; })

    if (__builtin_expect(wv < 8, 1)) {
        if (__builtin_expect(wv >= 4, 0)) {
            RW_DECL;
            u32x4 d[32];
            if (w < 3) {
                const __amdgpu_buffer_rsrc_t rp = mkrs((const void*)((kargp)__builtin_amdgcn_kernarg_segment_ptr())[4 + w], FF * 2);
#pragma unroll
                for (int t = 0; t < 9; ++t) d[t] = __builtin_amdgcn_raw_buffer_load_b128(rp, lane * 16, t * 1024, 0);
#pragma unroll
                for (int t = 0; t < 9; ++t) *(LAS u32x4*)(L + L_PR + w * 9216 + t * 1024 + lane * 16) = d[t];
                lds_signal(L + L_FLAG + F_G0, lane);
                asm volatile("" :: "v"(hd[0]));
            } else {
                if (bi < 8) __builtin_amdgcn_raw_buffer_store_b32(tstart, rw, lane == 0 ? WS_T + bi * 4 : OOB, 0, AUX_SC1);
                const unsigned ep = hd[0].x + 1u;
                *(LAS u32x2*)(L + L_FLAG + F_TAG) = u32x2{ep & 0xfffu, (ep ^ (ep >> 12)) & 0xfffu};
                *(LAS unsigned*)(L + L_FLAG + F_EP) = ep;
                DRAIN();
                lds_signal(L + L_FLAG + F_WQ, lane);
                sleep_until(tstart + T_RD);
                int dt = (int)(__builtin_amdgcn_raw_buffer_load_b32(rw, WS_T + (lane & 7) * 4, 0, AUX_SC1) - tstart);
                dt = (dt > -400 && dt < 400) ? dt : 0;
                float mean = (float)dt, mx = mean;
                mean += dpp<XOR1>(mean); mean += dpp<XOR2>(mean); mean += dpp<MIR8>(mean);
                mx = fmaxf(mx, dpp<XOR1>(mx)); mx = fmaxf(mx, dpp<XOR2>(mx)); mx = fmaxf(mx, dpp<MIR8>(mx));
                const float sh = fmaxf(mean * 0.125f, mx - (float)CATCH);
                *(LAS unsigned*)(L + L_FLAG + F_TD) = (unsigned)fminf(fmaxf(((float)(T_END - T0_V) + sh) * (8.0f / 31.0f), 80.f), 170.f);
                lds_signal(L + L_FLAG + F_TV, lane);
            }
            __builtin_amdgcn_s_setprio(1);
            const __amdgpu_buffer_rsrc_t rs = mkrs(vc + KV_OFF, KV_SPAN);
            unsigned dl = tstart + T0_V;
            sleep_until(dl - 20u);
            lds_wait<1>(L + L_FLAG + F_TV, 1u);
            const unsigned tau = __builtin_amdgcn_readfirstlane(*(LAS unsigned*)(L + L_FLAG + F_TD));
            dl <<= 3;
            unsigned sel[2][4];
#pragma unroll
            for (int hs = 0; hs < 2; ++hs) {
                const unsigned bb = (unsigned)(2 * hs) | ((unsigned)(2 * hs + 1) << 8);
                const unsigned on = (c & 1) ? ((bb << 16) | 0x0c0cu) : (0x0c0c0000u | bb);
#pragma unroll
                for (int dd = 0; dd < 4; ++dd) sel[hs][dd] = (((c & 7) >> 1) == dd) ? on : 0x0c0c0c0cu;
            }
            int oo = WS_O + ((kvh * 2 + (g >> 1)) * 16 + r) * 256 + c * 16 + (g & 1) * 8;
            float wt;
            u32x4 P[4];
            f32x4 acc = {0.f, 0.f, 0.f, 0.f};
#define PV_STEP(j_) do { const int dw = ((j_) & 7) * 2 + ((j_) >> 4), hs = ((j_) >> 3) & 1; const unsigned pv = P[dw >> 2][dw & 3]; u32x4 a; \
                _Pragma("unroll") for (int dd = 0; dd < 4; ++dd) a[dd] = __builtin_amdgcn_perm(pv, pv, sel[hs][dd]); acc = mfma32(a, d[j_], acc); } while (0)
#pragma unroll
            for (int jj = 0; jj < 32; ++jj) {
                pace8(dl, tau);
                d[jj] = __builtin_amdgcn_raw_buffer_load_b128(rs, KV_LANE + (jj & 3) * 1024, (jj >> 2) * 65536, AUX_NT);
                SB();
                if (jj == 31) lds_signal(L + L_FLAG + F_VI, lane);
                if (jj == PV0) {
                    lds_wait<1>(L + L_FLAG + F_P, 4u);
                    const f32x4 ma = *(LAS f32x4*)(L + L_ML + (g >> 1) * 32), mb = *(LAS f32x4*)(L + L_ML + (g >> 1) * 32 + 16);
                    const float mw = *(LAS float*)(L + L_ML + (g >> 1) * 32 + w * 8);
                    wt = __builtin_amdgcn_exp2f(mw - fmaxf(fmaxf(ma.x, ma.z), fmaxf(mb.x, mb.z)));
#pragma unroll
                    for (int t = 0; t < 4; ++t) P[t] = *(LAS u32x4*)(L + L_P + w * 512 + (c >> 3) * 256 + g * 64 + t * 16);
                    asm volatile("" : "+v"(wt), "+v"(oo));
                }
                if (jj > PV0 && jj < 31) {
#pragma unroll
                    for (int t = 0; t < PVN; ++t) PV_STEP((jj - PV0 - 1) * PVN + t);
                }
                SB();
            }
#pragma unroll
            for (int jj = (30 - PV0) * PVN; jj < 32; ++jj) PV_STEP(jj);
            acc *= wt;
            *(LAS f32x4*)(L + L_H + w * 1024 + lane * 16) = acc;
            WG_BARRIER();
            if (w == 0) {
                acc += *(LAS f32x4*)(L + L_H + 1024 + lane * 16);
                acc += *(LAS f32x4*)(L + L_H + 2048 + lane * 16) + *(LAS f32x4*)(L + L_H + 3072 + lane * 16);
                u32x2 o; o.x = min(pk_bf16(acc.x, acc.y), SENT - 1); o.y = min(pk_bf16(acc.z, acc.w), SENT - 1);
                __builtin_amdgcn_raw_buffer_store_b64(o, rw, oo, 0, AUX_SC1);
                if ((g >> 1) == (r >> 3)) *(LAS u32x2*)(L + L_O + r * 272 + c * 16 + (g & 1) * 8) = o;
                asm volatile("s_waitcnt lgkmcnt(0)" ::: "memory");
            }
        } else {
            u32x4 d[32];
            const __amdgpu_buffer_rsrc_t rs = mkrs(kc + KV_OFF, KV_SPAN);
#pragma unroll
            for (int jj = 0; jj < KHEAD; ++jj) d[jj] = hd[jj];
            unsigned dl = tstart + T0_K + KHEAD * TAU;
#pragma unroll
            for (int jj = KHEAD; jj < 32; ++jj) { pace(dl); d[jj] = __builtin_amdgcn_raw_buffer_load_b128(rs, KV_LANE + (jj & 3) * 1024, (jj >> 2) * 65536, AUX_NT); SB(); }
            RW_DECL;
            lds_wait<2>(L + L_FLAG + F_Q, 1u);
            const u32x4 qown = *(LAS u32x4*)(L + L_Q + (c >> 3) * 256 + c * 16);
            const u32x4 qoth = *(LAS u32x4*)(L + L_Q + ((c >> 3) ^ 1) * 256 + c * 16);
            float s[4];
#pragma unroll
            for (int q8 = 0; q8 < 4; ++q8) {
                float y[8];
#pragma unroll
                for (int t = 0; t < 8; ++t) {
                    const u32x4 kk = d[q8 * 8 + t];
                    float xo = 0.f, xt = 0.f;
#pragma unroll
                    for (int e = 0; e < 4; ++e) { xo = dot2(kk[e], qown[e], xo); xt = dot2(kk[e], qoth[e], xt); }
                    y[t] = xo + dpp<ROR8>(xt);
                }
                float z[4], u[2];
#pragma unroll
                for (int t = 0; t < 4; ++t) { const float a = y[t] + dpp<MIR8>(y[t]), b = y[t + 4] + dpp<MIR8>(y[t + 4]); z[t] = (c & 4) ? b : a; }
#pragma unroll
                for (int t = 0; t < 2; ++t) { const float a = z[t] + dpp<XOR2>(z[t]), b = z[t + 2] + dpp<XOR2>(z[t + 2]); u[t] = (c & 2) ? b : a; }
                { const float a = u[0] + dpp<XOR1>(u[0]), b = u[1] + dpp<XOR1>(u[1]); s[q8] = rbf(rbf((c & 1) ? b : a) * 0.08838834764831845f) * 1.4426950408889634f; }
            }
            float m = fmaxf(fmaxf(s[0], s[1]), fmaxf(s[2], s[3]));
            m = fmaxf(m, dpp<XOR1>(m)); m = fmaxf(m, dpp<XOR2>(m)); m = fmaxf(m, dpp<MIR8>(m));
            m = xrow_max(m);
            u32x2 pp;
            pp.x = pk_bf16(__builtin_amdgcn_exp2f(s[0] - m), __builtin_amdgcn_exp2f(s[1] - m));
            pp.y = pk_bf16(__builtin_amdgcn_exp2f(s[2] - m), __builtin_amdgcn_exp2f(s[3] - m));
            *(LAS u32x2*)(L + L_P + w * 512 + (c >> 3) * 256 + g * 64 + (c & 7) * 8) = pp;
            float l = (lo_f(pp.x) + hi_f(pp.x)) + (lo_f(pp.y) + hi_f(pp.y));
            l += dpp<XOR1>(l); l += dpp<XOR2>(l); l += dpp<MIR8>(l);
            l = xrow_sum(l);
            if ((lane & 55) == 0) *(LAS f32x2*)(L + L_ML + (c >> 3) * 32 + w * 8) = f32x2{m, l};
            lds_signal(L + L_FLAG + F_P, lane);
            u32x4 wr[5];
            const __amdgpu_buffer_rsrc_t ro = mkrs(LATE_ARG(const unsigned char*, 9) + (size_t)(kvh * 256 + (r >> 3) * 128) * 1152 + (r & 7) * 144, 127 * 1152 + 144);
            int wof[5];
#pragma unroll
            for (int t = 0; t < 5; ++t) { const int ch = (t * 4 + w) * 64 + lane; wof[t] = (ch / 9) * 1152 + (ch % 9) * 16; asm volatile("" : "+v"(wof[t])); }
            int so = (c & 1) == 0 ? WS_S + (((kvh * 2 + (r >> 3)) * 8 + (r & 7)) * 4 + g) * 320 + w * 64 + c * 4 : OOB;
            u32x2 tag = *(LAS u32x2*)(L + L_FLAG + F_TAG);
            asm volatile("" : "+v"(so), "+v"(tag));
            lds_wait<1>(L + L_FLAG + F_VI, 4u);
            dl = (unsigned)__builtin_amdgcn_s_memrealtime();
#pragma unroll
            for (int t = 0; t < 5; ++t) { pace(dl); wr[t] = __builtin_amdgcn_raw_buffer_load_b128(ro, wof[t], 0, 0); SB(); }
            __builtin_amdgcn_s_barrier();
#pragma unroll
            for (int t = 0; t < 5; ++t) *(LAS u32x4*)(L + L_WO + (t * 4 + w) * 1024 + lane * 16) = wr[t];
            lds_signal(L + L_FLAG + F_WO, lane);
            out_tile(L, rw, w, g, c, so, tag);
        }
    } else if (wv == 8) {
        const int voff = (lane >> 3) * 576 + (lane & 7) * 16;
        RW_DECL;
        {
            float xv[40];
            float sum = 0.f;
#pragma unroll
            for (int t = 0; t < 5; ++t)
#pragma unroll
                for (int e = 0; e < 4; ++e) { const unsigned z = (t < 4 || (c & 7) < 4) ? hd[t][e] : 0u; xv[8 * t + 2 * e] = lo_f(z); xv[8 * t + 2 * e + 1] = hi_f(z); }
#pragma unroll
            for (int t = 0; t < 40; ++t) sum += xv[t];
            const float mean = row_sum(sum) * (1.0f / DM);
            const float mean4 = (c & 7) < 4 ? mean : 0.f;
            float var = 0.f;
#pragma unroll
            for (int t = 0; t < 40; ++t) { const float dd = xv[t] - (t < 32 ? mean : mean4); var = fmaf(dd, dd, var); }
            const float rstd = __builtin_amdgcn_rcpf(rbf(__builtin_amdgcn_sqrtf(rbf(rbf(row_sum(var) * (1.0f / DM)) + 1e-5f))));
            const float mb = rbf(mean);
#pragma unroll
            for (int t = 0; t < 5; ++t) {
                u32x4 o;
#pragma unroll
                for (int e = 0; e < 4; ++e) {
                    const unsigned dp = pk_bf16(xv[8 * t + 2 * e] - mb, xv[8 * t + 2 * e + 1] - mb);
                    o[e] = pk_bf16(lo_f(dp) * rstd, hi_f(dp) * rstd);
                }
                if (t < 4 || (c & 7) < 4) *(LAS u32x4*)(L + L_XN + voff + t * 128) = o;
            }
            lds_signal(L + L_FLAG + F_G0, lane);
        }
        lds_wait<1>(L + L_FLAG + F_WQ, 4u);
        const u32x2 tag = *(LAS u32x2*)(L + L_FLAG + F_TAG);
        {
            LAS unsigned char* xa = L + L_XN + (c & 3) * 1152 + (r & 3) * 288;
            LAS unsigned char* wb = L + L_WQ + (c >> 2) * 128 + (c & 3) * 8;
            f32x4 acc[4];
#pragma unroll
            for (int t = 0; t < 4; ++t) acc[t] = f32x4{0.f, 0.f, 0.f, 0.f};
#pragma unroll
            for (int s = 0; s < 4; ++s) {
                const u32x4 a = *(LAS u32x4*)(xa + s * 64 + g * 16);
#pragma unroll
                for (int t = 0; t < 4; ++t) {
                    const u32x2 b0 = tr_read(wb + (s * 32 + g * 8) * 128 + t * 32), b1_ = tr_read(wb + (s * 32 + g * 8 + 4) * 128 + t * 32);
                    acc[t] = mfma32(a, u32x4{b0.x, b0.y, b1_.x, b1_.y}, acc[t]);
                }
            }
            const u32x2 a4 = *(LAS u32x2*)(xa + 256 + g * 8);
#pragma unroll
            for (int t = 0; t < 4; ++t) acc[t] = mfma16(a4, tr_read(wb + (128 + g * 4) * 128 + t * 32), acc[t]);
            float pv[4];
#pragma unroll
            for (int t = 0; t < 4; ++t) pv[t] = g == 0 ? acc[t].x : (g == 1 ? acc[t].y : (g == 2 ? acc[t].z : acc[t].w));
            const int qo = WS_Q + ((kvh * 4 + g) * 4 + (r & 3)) * 1024 + (r >> 2) * 256 + c * 16;
            __builtin_amdgcn_raw_buffer_store_b128(u32x4{pk20(pv[0], tag.x), pk20(pv[1], tag.y), pk20(pv[2], tag.x), pk20(pv[3], tag.y)}, rw, qo, 0, AUX_SC1);
        }
        __builtin_amdgcn_s_sleep(DLY_Q);
        {
            u32x4 gq[4];
            const int qi = WS_Q + (kvh * 4 + (r >> 2)) * 4096 + lane * 16;
#pragma unroll 1
            for (unsigned spins = 0; spins < SPIN_CAP; ++spins) {
                MEMBAR();
                unsigned bad = 0;
#pragma unroll
                for (int t = 0; t < 4; ++t) gq[t] = __builtin_amdgcn_raw_buffer_load_b128(rw, qi, t * 1024, AUX_SC1);
#pragma unroll
                for (int t = 0; t < 4; ++t) bad |= bad20(gq[t], tag);
                if (__all((bad & 0xfffu) == 0u)) break;
                __builtin_amdgcn_s_sleep(8);
            }
#pragma unroll
            for (int t = 0; t < 4; ++t)
                *(LAS unsigned short*)(L + L_Q + (g * 64 + t * 16 + c) * 2) = (unsigned short)pk_bf16((un20(gq[0][t]) + un20(gq[1][t])) + (un20(gq[2][t]) + un20(gq[3][t])), 0.f);
            lds_signal(L + L_FLAG + F_Q, lane);
        }
        lds_wait<4>(L + L_FLAG + F_P, 4u);
        {
            const f32x4 a = *(LAS f32x4*)(L + L_ML + (lane & 1) * 32), b = *(LAS f32x4*)(L + L_ML + (lane & 1) * 32 + 16);
            const float M = fmaxf(fmaxf(a.x, a.z), fmaxf(b.x, b.z));
            const float Ls = __builtin_amdgcn_exp2f(a.x - M) * a.y + __builtin_amdgcn_exp2f(a.z - M) * a.w + __builtin_amdgcn_exp2f(b.x - M) * b.y +
                             __builtin_amdgcn_exp2f(b.z - M) * b.w;
            u32x2 o; o.x = min(__float_as_uint(M), SENT - 1); o.y = min(__float_as_uint(Ls), SENT - 1);
            __builtin_amdgcn_raw_buffer_store_b64(o, rw, lane < 2 ? WS_ML + ((kvh * 2 + lane) * 16 + r) * 8 : OOB, 0, AUX_SC1);
        }
        int oi[4], ol[4];
#pragma unroll
        for (int t = 0; t < 4; ++t) {
            const bool own = t * 4 + g == r;
            oi[t] = own ? OOB : WS_O + (kvh * 2 + (r >> 3)) * 4096 + t * 1024 + lane * 16;
            ol[t] = own ? L_H : L_O + (t * 4 + g) * 272 + c * 16;
            asm volatile("" : "+v"(oi[t]), "+v"(ol[t]));
        }
        __builtin_amdgcn_s_barrier();
        __builtin_amdgcn_s_sleep(DLY_O);
        {
            u32x4 ov[4];
#pragma unroll 1
            for (unsigned spins = 0; spins < SPIN_CAP; ++spins) {
                MEMBAR();
#pragma unroll
                for (int t = 0; t < 4; ++t) ov[t] = __builtin_amdgcn_raw_buffer_load_b128(rw, oi[t], 0, AUX_SC1);
                unsigned mx = 0;
#pragma unroll
                for (int t = 0; t < 4; ++t) { mx = max(mx, max(max(ov[t].x, ov[t].y), max(ov[t].z, ov[t].w))); *(LAS u32x4*)(L + ol[t]) = ov[t]; }
                if (__all(mx != SENT)) break;
            }
        }
        WG_BARRIER();
    } else {
        const int hw = wv - 9;
        RW_DECL;
        const int ci = (bi & 7) * 32 + (bi >> 3);
#pragma unroll
        for (int t = 0; t < 6; ++t) *(LAS u32x4*)(L + L_WQ + (hw * 6 + t) * 1024 + lane * 16) = hd[t];
        lds_signal(L + L_FLAG + F_WQ, lane);
        const int cq = lane & 3, oj = cq * 4 + (((g & 1) << 1) | (g >> 1)) - ((ci * 9) & 3);
        const bool ov = (lane & 12) == 0 && (unsigned)oj < 9u && cq < 3;
        const int oi = ov ? oj : 0;
        __builtin_amdgcn_s_setprio(0);
        lds_wait<2>(L + L_FLAG + F_G0, 4u);
        const int xl = lane < 36 ? lane : 0;
        const u32x4 xa = *(LAS u32x4*)(L + L_XN + (ci >> 6) * 1152 + xl * 32), xb = *(LAS u32x4*)(L + L_XN + (ci >> 6) * 1152 + xl * 32 + 16);
        const float msk = lane < 36 ? 1.0f : 0.0f;
        float xv[16];
#pragma unroll
        for (int e = 0; e < 4; ++e) { xv[2 * e] = lo_f(xa[e]); xv[2 * e + 1] = hi_f(xa[e]); xv[8 + 2 * e] = lo_f(xb[e]); xv[8 + 2 * e + 1] = hi_f(xb[e]); }
        float sum = 0.f;
#pragma unroll
        for (int e = 0; e < 16; ++e) sum += xv[e];
        const float mean = xrow_sum(row_sum(sum * msk)) * (1.0f / DM);
        float var = 0.f;
#pragma unroll
        for (int e = 0; e < 16; ++e) { const float dd = xv[e] - mean; var = fmaf(dd, dd, var); }
        const float rstd = __builtin_amdgcn_rcpf(rbf(__builtin_amdgcn_sqrtf(rbf(rbf(xrow_sum(row_sum(var * msk)) * (1.0f / DM)) + 1e-5f))));
        const float mb = rbf(mean);
        f32x2 vw1[12], vb1[12], vw2[12];
#pragma unroll
        for (int t = 0; t < 3; ++t) {
            const u32x4 r1 = *(LAS u32x4*)(L + L_PR + (hw * 3 + t) * 1024 + lane * 16), rb = *(LAS u32x4*)(L + L_PR + 9216 + (hw * 3 + t) * 1024 + lane * 16),
                        r2 = *(LAS u32x4*)(L + L_PR + 18432 + (hw * 3 + t) * 1024 + lane * 16);
#pragma unroll
            for (int e = 0; e < 4; ++e) {
                vw1[t * 4 + e] = f32x2{lo_f(r1[e]), hi_f(r1[e])};
                vb1[t * 4 + e] = f32x2{lo_f(rb[e]), hi_f(rb[e])};
                vw2[t * 4 + e] = f32x2{lo_f(r2[e]), hi_f(r2[e])};
            }
        }
        constexpr float C1 = -2.0f * 0.7978845608028654f * 1.4426950408889634f, C2 = C1 * 0.044715f;
        LAS unsigned short* xs = (LAS unsigned short*)(L + L_XN + ci * 18);
        unsigned xn_next = xs[0];
        float ysum = 0.f, prev = 0.f;
#pragma unroll 1
        for (int o = 0; o < 9; ++o) {
            const float h = rbf(rbf(lo_f(xn_next) - mb) * rstd);
            xn_next = xs[o + 1];
            f32x2 u[12], a[12];
#pragma unroll
            for (int t = 0; t < 12; ++t) u[t] = vw1[t] * h + vb1[t];
#pragma unroll
            for (int t = 0; t < 12; ++t) a[t] = u[t] * u[t];
            const float tot = xrow_sum(row_sum(prev));
            ysum = lane + 1 == o ? tot : ysum;
#pragma unroll
            for (int t = 0; t < 12; ++t) a[t] = a[t] * C2 + C1;
#pragma unroll
            for (int t = 0; t < 12; ++t) a[t] = a[t] * u[t];
            SB();
#pragma unroll
            for (int t = 0; t < 12; ++t) { a[t].x = __builtin_amdgcn_exp2f(a[t].x); a[t].y = __builtin_amdgcn_exp2f(a[t].y); u[t] = u[t] * vw2[t]; }
            SB();
#pragma unroll
            for (int t = 0; t < 12; ++t) a[t] += 1.0f;
            SB();
#pragma unroll
            for (int t = 0; t < 12; ++t) { a[t].x = __builtin_amdgcn_rcpf(a[t].x); a[t].y = __builtin_amdgcn_rcpf(a[t].y); }
            SB();
            f32x2 s0 = u[0] * a[0], s1 = u[1] * a[1], s2 = u[2] * a[2];
#pragma unroll
            for (int t = 3; t < 12; t += 3) { s0 += u[t] * a[t]; s1 += u[t + 1] * a[t + 1]; s2 += u[t + 2] * a[t + 2]; }
            s0 += s1 + s2;
            prev = s0.x + s0.y;
        }
        { const float tot = xrow_sum(row_sum(prev)); ysum = lane == 8 ? tot : ysum; }
        if (lane < 9) *(LAS float*)(L + L_Y + hw * 64 + lane * 4) = ysum;
        __builtin_amdgcn_s_setprio(3);
        unsigned xraw = 0, braw = 0;
        if (hw == 0) { xraw = *(const GAS unsigned short*)(xin + (ci * 9 + oi) * 2); braw = *LATE_ARG(const GAS unsigned short*, 10); }
        WG_BARRIER();
        const u32x2 tag = *(LAS u32x2*)(L + L_FLAG + F_TAG);
        if (hw == 0) {
            const float y0 = *(LAS float*)(L + L_Y + oi * 4) + *(LAS float*)(L + L_Y + 64 + oi * 4) + *(LAS float*)(L + L_Y + 128 + oi * 4);
            const float y = rbf(rbf(y0) + lo_f(braw));
            float gt = rbf(y * __builtin_amdgcn_rcpf(1.0f + __builtin_amdgcn_exp2f(-1.4426950408889634f * y)));
            float xr = lo_f(xraw);
            int si = cq < 3 ? WS_S + (((lane >> 2) * 8 + ((ci >> 3) & 7)) * 4 + (ci >> 6)) * 320 + ((((ci & 7) * 9) >> 2) + cq) * 16 : OOB;
            int oo = ov ? (ci * 9 + oi) * 2 : OOB;
            const __amdgpu_buffer_rsrc_t rout = mkrs(LATE_ARG(const void*, 8), 4 * DM * 2);
            unsigned want = cq < 3 ? 0xfffu : 0u;
            const unsigned ep = *(LAS unsigned*)(L + L_FLAG + F_EP);
            asm volatile("" : "+v"(xr), "+v"(gt), "+v"(si), "+v"(oo), "+v"(want));
            __builtin_amdgcn_s_barrier();
            __builtin_amdgcn_s_sleep(DLY_S);
            u32x4 sa, sb;
#pragma unroll 1
            for (unsigned spins = 0; spins < SPIN_CAP; ++spins) {
                MEMBAR();
                sa = __builtin_amdgcn_raw_buffer_load_b128(rw, si, 0, AUX_SC1);
                sb = __builtin_amdgcn_raw_buffer_load_b128(rw, si, 16 * 32 * 320, AUX_SC1);
                if (__all(((bad20(sa, tag) | bad20(sb, tag)) & want) == 0u)) break;
            }
            float s = swap16_sum(swap32_sum(un20(sa.x) + un20(sb.x), un20(sa.y) + un20(sb.y)), swap32_sum(un20(sa.z) + un20(sb.z), un20(sa.w) + un20(sb.w)));
            s += dpp<ROR8>(s);
            s += dpp<ROR4>(s);
            __builtin_amdgcn_raw_buffer_store_b16((unsigned short)pk_bf16(xr + rbf(rbf(s) * gt), 0.f), rout, oo, 0, 0);
            if (bi == 0)
                __builtin_amdgcn_raw_buffer_store_b32(ep, rw, lane == 0 ? WS_EPOCH : OOB, 0, AUX_SC1);
        } else if (hw == 1) {
            const int so = (c & 9) == 0 ? WS_S + (((kvh * 2 + (r >> 3)) * 8 + (r & 7)) * 4 + g) * 320 + 256 + c * 4 : OOB;
            out_tile(L, rw, 4, g, c, so, tag);
        } else {
            const int mi = WS_ML + ((kvh * 2 + (r >> 3)) * 16 + c) * 8;
            u32x2 ml;
            __builtin_amdgcn_s_sleep(DLY_ML);
#pragma unroll 1
            for (unsigned spins = 0; spins < SPIN_CAP; ++spins) {
                MEMBAR();
                ml = __builtin_amdgcn_raw_buffer_load_b64(rw, mi, 0, AUX_SC1);
                if (__all(max(ml.x, ml.y) != SENT)) break;
                __builtin_amdgcn_s_sleep(2);
            }
            const float mp = __uint_as_float(ml.x);
            float M = fmaxf(mp, dpp<XOR1>(mp)); M = fmaxf(M, dpp<XOR2>(M));
            const float ew = __builtin_amdgcn_exp2f(mp - M);
            float Ls = ew * __uint_as_float(ml.y); Ls += dpp<XOR1>(Ls); Ls += dpp<XOR2>(Ls);
            *(LAS float*)(L + L_WT + c * 4) = ew * __builtin_amdgcn_rcpf(Ls);
            asm volatile("s_waitcnt lgkmcnt(0)" ::: "memory");
        }
    }
}

extern "C" void kernel_launch(void* const* d_in, const int* in_sizes, int n_in, void* d_out, int out_size, void* d_ws, size_t ws_size,
                              hipStream_t stream) {
    typedef const unsigned char* P;
    fused_decode<<<256, 768, 0, stream>>>((P)d_in[1], (P)d_in[2], (P)d_in[0], (P)d_in[3], (P)d_in[6], (P)d_in[7], (P)d_in[8], (unsigned char*)d_ws,
                                          (unsigned short*)d_out, (P)d_in[4], (P)d_in[9]);
}
```
